```python
import torch

def _softmax(x, axis=-1):
    return torch.softmax(x, dim=axis)
def _gelu(x):
    return torch.nn.functional.gelu(x, approximate="tanh")
def _silu(x):
    return torch.nn.functional.silu(x)
def _sigmoid(x):
    return torch.sigmoid(x)
def _layernorm(x, axis=-1, eps=1e-5):
    m = x.mean(dim=axis, keepdim=True)
    v = x.var(dim=axis, keepdim=True, unbiased=False)
    return (x - m) / torch.sqrt(v + eps)
def _scatter_add(tgt, idx, src, axis=0):
    return tgt.clone().index_add_(axis, idx, src)
def _segment_sum(src, seg, n):
    out = torch.zeros((n,) + src.shape[1:], dtype=src.dtype)
    return out.index_add_(0, seg, src)
def _pad(x, pw):
    flat = tuple(v for pair in reversed(pw) for v in pair)
    return torch.nn.functional.pad(x, flat)

kernel_name = 'recurrent_scan__mha_block_cached__glu_gated_swiglu__decode'

def setup_inputs():

    _w = (lambda a: (lambda t: t.to(torch.bfloat16) if t.is_floating_point() else t)(torch.as_tensor(a)))
    def _A_setup():
        B, Sq, D = 4, 1, 512
        H, Sk, Dh = 32, 8192, 128
        return {
            "x": _w((lambda *s: torch.randn(*s, dtype=torch.bfloat16) * 0.2)(B, Sq, D)),
            "k_cache": _w((lambda *s: torch.randn(*s, dtype=torch.bfloat16) * 1.6)(B, H, Sk, Dh)),
            "v_cache": _w((lambda *s: torch.randn(*s, dtype=torch.bfloat16) * 1.6)(B, H, Sk, Dh)),
            "wq": _w((lambda *s: torch.randn(*s, dtype=torch.bfloat16) * 1.6)(D, H * Dh) * (D ** -0.5)),
            "wo": _w((lambda *s: torch.randn(*s, dtype=torch.bfloat16) * 1.6)(H * Dh, D) * ((H * Dh) ** -0.5)),
        }
    _A_in = _A_setup()

    def _B_setup():
        B, S, D, F = 4, 1, 512, 1792
        return {
            "x": _w((lambda *s: torch.randn(*s, dtype=torch.bfloat16) * 0.2)(B, S, D)),
            "w1": _w((lambda *s: torch.randn(*s, dtype=torch.bfloat16) * 0.8)(D, F) * (2.0 / D) ** 0.5),
            "w3": _w((lambda *s: torch.randn(*s, dtype=torch.bfloat16) * 0.8)(D, F) * (2.0 / D) ** 0.5),
            "w2": _w((lambda *s: torch.randn(*s, dtype=torch.bfloat16) * 0.8)(F, D) * (2.0 / F) ** 0.5),
        }
    _B_in = _B_setup()

    return {
        "A_x": _A_in["x"],
        "A_k_cache": _A_in["k_cache"],
        "A_v_cache": _A_in["v_cache"],
        "A_wq": _A_in["wq"],
        "A_wo": _A_in["wo"],
        "B_x": _B_in["x"],
        "B_w1": _B_in["w1"],
        "B_w3": _B_in["w3"],
        "B_w2": _B_in["w2"],
    }

def reference(A_x, A_k_cache, A_v_cache, A_wq, A_wo, B_x, B_w1, B_w3, B_w2):
    def _A_ref(x, k_cache, v_cache, wq, wo):
        B, Sq, D = x.shape
        H, Dh = 32, 128
        q = (lambda x, *d: x.permute(*d))((x @ wq).reshape(B, Sq, H, Dh), 0, 2, 1, 3)
        scores = torch.einsum("bhqd,bhkd->bhqk", q, k_cache) * (Dh ** -0.5)
        p = _softmax(scores, axis=-1)
        o = torch.einsum("bhqk,bhkd->bhqd", p, v_cache)
        return (lambda x, *d: x.permute(*d))(o, 0, 2, 1, 3).reshape(B, Sq, H * Dh) @ wo

    def _B_ref(x, w1, w3, w2):
        gate = torch.einsum("bsd,df->bsf", x, w1)
        up = torch.einsum("bsd,df->bsf", x, w3)
        h = _silu(gate) * up
        out = torch.einsum("bsf,fd->bsd", h, w2)
        return out

    x = A_x
    xn = _layernorm(x, axis=-1)
    s = _A_ref(xn, A_k_cache, A_v_cache, A_wq, A_wo)
    g = _silu(_B_ref(xn, B_w1, B_w3, B_w2))
    return x + s * g
```

```cpp
#include <hip/hip_runtime.h>
#include <hip/hip_bf16.h>
#include <cstdint>

using u32x4 = __attribute__((ext_vector_type(4))) unsigned;
using f32x4 = __attribute__((ext_vector_type(4))) float;
using bf16x2 = __attribute__((ext_vector_type(2))) __bf16;

constexpr int NB = 4, NH = 32, SK = 8192, DH = 128, DM = 512, FF = 1792;
constexpr int NWG = 256;
constexpr int KEYS = SK / 2;
constexpr int NSL = FF / 32;
#define U_ 4
#define DEPTH_ 2
constexpr int U = U_, DEPTH = DEPTH_;
constexpr int NSW = 4;
constexpr int TILE_B = NSW * U * 1024;
constexpr int ITERS = KEYS * DH * 2 / TILE_B;
static_assert(ITERS % DEPTH == 0, "");
constexpr float C2 = 0.088388347648318440f * 1.4426950408889634f;

constexpr size_t WS_Y = 0, WS_Z = WS_Y + (size_t)NWG * DM * 4, WS_ML = WS_Z + (size_t)NB * 64 * DM * 4;

__device__ __forceinline__ float dot2(unsigned a, unsigned b, float acc) {
  return __builtin_amdgcn_fdot2_f32_bf16(__builtin_bit_cast(bf16x2, a), __builtin_bit_cast(bf16x2, b), acc, false);
}
template <int CTRL> __device__ __forceinline__ float dpp(float x) {
  return __builtin_bit_cast(float, __builtin_amdgcn_mov_dpp(__builtin_bit_cast(int, x), CTRL, 0xf, 0xf, true));
}
__device__ __forceinline__ float row16_sum(float x) {
  x += dpp<0x128>(x); x += dpp<0x124>(x); x += dpp<0x122>(x); x += dpp<0x121>(x); return x;
}
__device__ __forceinline__ float xrow_sum(float x) {
  auto s = __builtin_amdgcn_permlane16_swap(__float_as_uint(x), __float_as_uint(x), false, false);
  x = __uint_as_float(s[0]) + __uint_as_float(s[1]);
  auto t = __builtin_amdgcn_permlane32_swap(__float_as_uint(x), __float_as_uint(x), false, false);
  return __uint_as_float(t[0]) + __uint_as_float(t[1]);
}
__device__ __forceinline__ float xrow_max(float x) {
  auto s = __builtin_amdgcn_permlane16_swap(__float_as_uint(x), __float_as_uint(x), false, false);
  x = fmaxf(__uint_as_float(s[0]), __uint_as_float(s[1]));
  auto t = __builtin_amdgcn_permlane32_swap(__float_as_uint(x), __float_as_uint(x), false, false);
  return fmaxf(__uint_as_float(t[0]), __uint_as_float(t[1]));
}
__device__ __forceinline__ float wave_sum(float x) { return xrow_sum(row16_sum(x)); }
__device__ __forceinline__ float bflo(unsigned w) { return __uint_as_float(w << 16); }
__device__ __forceinline__ float bfhi(unsigned w) { return __uint_as_float(w & 0xffff0000u); }
__device__ __forceinline__ unsigned cvtpk(float lo, float hi) {
  unsigned r; asm("v_cvt_pk_bf16_f32 %0, %1, %2" : "=v"(r) : "v"(lo), "v"(hi)); return r;
}
__device__ __forceinline__ float rbf(float x) { return bflo(cvtpk(x, x)); }
__device__ __forceinline__ __amdgpu_buffer_rsrc_t mkrsrc(const void* p, unsigned bytes) {
  return __builtin_amdgcn_make_buffer_rsrc(const_cast<void*>(p), 0, bytes, 0x00020000);
}
__device__ __forceinline__ void wg_barrier() { asm volatile("s_waitcnt lgkmcnt(0)\n\ts_barrier" ::: "memory"); }
__device__ __forceinline__ float silu(float x) { return x / (1.f + __expf(-x)); }

#define SB() __builtin_amdgcn_sched_barrier(0)
struct Lds {
  float xn[4][DM];
  float qp[4][DH];
  float op[NSW][DH + 4];
  float yp[4][DM];
  float zp[4][DM];
};

__global__ __launch_bounds__(512) void attn_main(const unsigned short* __restrict__ x, const unsigned short* __restrict__ kc,
    const unsigned short* __restrict__ vc, const unsigned short* __restrict__ wq, const unsigned short* __restrict__ wo,
    const unsigned short* __restrict__ w1, const unsigned short* __restrict__ w3, const unsigned short* __restrict__ w2,
    unsigned char* __restrict__ ws) {
  __shared__ __attribute__((aligned(16))) Lds L;
  const int w = blockIdx.x, j = w >> 3, h = (w & 7) * 4 + (j >> 3), b = (j & 7) >> 1, half = j & 1;
  const int tid = threadIdx.x, lane = tid & 63, wave = __builtin_amdgcn_readfirstlane(tid >> 6);
  const int r = lane >> 4, c = lane & 15;
  const int slot = (b * NH + h) * 2 + half;
  const int sl = h * 2 + half;
  if (wave < NSW) {
    const size_t kvoff = ((size_t)(b * NH + h) * SK + (size_t)half * KEYS) * DH;
    const auto rk = mkrsrc(kc + kvoff, KEYS * DH * 2), rv = mkrsrc(vc + kvoff, KEYS * DH * 2);
    const unsigned vo = lane * 16 + wave * U * 1024;
    u32x4 kb[DEPTH][U], vb[DEPTH][U];
#pragma unroll
    for (int d = 0; d < DEPTH; ++d) {
#pragma unroll
      for (int u = 0; u < U; ++u) kb[d][u] = __builtin_amdgcn_raw_buffer_load_b128(rk, vo + u * 1024, d * TILE_B, 2);
#pragma unroll
      for (int u = 0; u < U; ++u) vb[d][u] = __builtin_amdgcn_raw_buffer_load_b128(rv, vo + u * 1024, d * TILE_B, 2);
    }
    wg_barrier();
    unsigned q[4];
    {
      f32x4 a0 = {0, 0, 0, 0}, a1 = {0, 0, 0, 0};
#pragma unroll
      for (int a = 0; a < 4; ++a) { a0 += *(const f32x4*)&L.qp[a][8 * c]; a1 += *(const f32x4*)&L.qp[a][8 * c + 4]; }
      q[0] = cvtpk(a0[0], a0[1]); q[1] = cvtpk(a0[2], a0[3]); q[2] = cvtpk(a1[0], a1[1]); q[3] = cvtpk(a1[2], a1[3]);
    }
    float m = -INFINITY, l = 0.f, acc[8];
#pragma unroll
    for (int k = 0; k < 8; ++k) acc[k] = 0.f;
    for (int it = 0; it < ITERS; it += DEPTH) {
#pragma unroll
      for (int d = 0; d < DEPTH; ++d) {
        const unsigned so = (unsigned)(it + d + DEPTH) * TILE_B;
        float s[U];
#pragma unroll
        for (int u = 0; u < U; ++u) {
          float t = dot2(kb[d][u][0], q[0], 0.f);
          t = dot2(kb[d][u][1], q[1], t); t = dot2(kb[d][u][2], q[2], t); s[u] = dot2(kb[d][u][3], q[3], t);
        }
        SB();
#pragma unroll
        for (int u = 0; u < U; ++u) kb[d][u] = __builtin_amdgcn_raw_buffer_load_b128(rk, vo + u * 1024, so, 2);
        SB();
        float mx = m;
#pragma unroll
        for (int u = 0; u < U; ++u) { s[u] = row16_sum(s[u]); mx = fmaxf(mx, s[u]); }
        const float mc = -mx * C2;
        const float alpha = __builtin_amdgcn_exp2f(fmaf(m, C2, mc));
        m = mx;
        float ps = 0.f;
#pragma unroll
        for (int u = 0; u < U; ++u) { s[u] = __builtin_amdgcn_exp2f(fmaf(s[u], C2, mc)); ps += s[u]; }
        l = fmaf(l, alpha, ps);
#pragma unroll
        for (int k = 0; k < 8; ++k) acc[k] *= alpha;
#pragma unroll
        for (int u = 0; u < U; ++u) {
#pragma unroll
          for (int k = 0; k < 4; ++k) {
            acc[2 * k] = fmaf(s[u], bflo(vb[d][u][k]), acc[2 * k]);
            acc[2 * k + 1] = fmaf(s[u], bfhi(vb[d][u][k]), acc[2 * k + 1]);
          }
        }
#pragma unroll
        for (int k = 0; k < 8; ++k) asm volatile("" : "+v"(acc[k]));
        SB();
#pragma unroll
        for (int u = 0; u < U; ++u) vb[d][u] = __builtin_amdgcn_raw_buffer_load_b128(rv, vo + u * 1024, so, 2);
        SB();
      }
    }
    const float M = xrow_max(m), f = __builtin_amdgcn_exp2f((m - M) * C2);
    l = xrow_sum(l * f);
#pragma unroll
    for (int k = 0; k < 8; ++k) acc[k] = xrow_sum(acc[k] * f);
    if (r == 0) {
      *(f32x4*)&L.op[wave][8 * c] = f32x4{acc[0], acc[1], acc[2], acc[3]};
      *(f32x4*)&L.op[wave][8 * c + 4] = f32x4{acc[4], acc[5], acc[6], acc[7]};
      if (c == 0) { L.op[wave][DH] = M; L.op[wave][DH + 1] = l; }
    }
    wg_barrier();
    if (tid < 128) {
      f32x4 z = *(const f32x4*)&L.zp[0][4 * tid];
#pragma unroll
      for (int a = 1; a < 4; ++a) z += *(const f32x4*)&L.zp[a][4 * tid];
      *(f32x4*)(ws + WS_Z + ((size_t)(b * 64 + sl) * DM + 4 * tid) * 4) = z;
    }
    wg_barrier();
    if (tid < 128) {
      f32x4 y = *(const f32x4*)&L.yp[0][4 * tid];
#pragma unroll
      for (int a = 1; a < 4; ++a) y += *(const f32x4*)&L.yp[a][4 * tid];
      *(f32x4*)(ws + WS_Y + ((size_t)slot * DM + 4 * tid) * 4) = y;
    }
  } else {
    const int a = wave - NSW;
    const u32x4 xv = *(const u32x4*)(x + b * DM + lane * 8);
    u32x4 wr[32];
    {
      const unsigned short* p = wq + (size_t)(128 * a + 32 * r) * (NH * DH) + h * DH + 8 * c;
#pragma unroll
      for (int i = 0; i < 32; ++i) wr[i] = *(const u32x4*)(p + (size_t)i * (NH * DH));
      SB();
    }
    {
      float xf[8];
#pragma unroll
      for (int k = 0; k < 4; ++k) { xf[2 * k] = bflo(xv[k]); xf[2 * k + 1] = bfhi(xv[k]); }
      float s = 0.f;
#pragma unroll
      for (int k = 0; k < 8; ++k) s += xf[k];
      const float mean = wave_sum(s) * (1.f / DM);
      float v = 0.f;
#pragma unroll
      for (int k = 0; k < 8; ++k) { xf[k] -= mean; v = fmaf(xf[k], xf[k], v); }
      const float rs = 1.f / sqrtf(wave_sum(v) * (1.f / DM) + 1e-5f);
      *(f32x4*)&L.xn[a][8 * lane] = f32x4{rbf(xf[0] * rs), rbf(xf[1] * rs), rbf(xf[2] * rs), rbf(xf[3] * rs)};
      *(f32x4*)&L.xn[a][8 * lane + 4] = f32x4{rbf(xf[4] * rs), rbf(xf[5] * rs), rbf(xf[6] * rs), rbf(xf[7] * rs)};
    }
    __builtin_amdgcn_wave_barrier();
    {
      float acc[8];
#pragma unroll
      for (int k = 0; k < 8; ++k) acc[k] = 0.f;
#pragma unroll
      for (int i4 = 0; i4 < 8; ++i4) {
        const f32x4 xn = *(const f32x4*)&L.xn[a][128 * a + 32 * r + 4 * i4];
#pragma unroll
        for (int e = 0; e < 4; ++e)
#pragma unroll
          for (int k = 0; k < 4; ++k) {
            acc[2 * k] = fmaf(xn[e], bflo(wr[4 * i4 + e][k]), acc[2 * k]);
            acc[2 * k + 1] = fmaf(xn[e], bfhi(wr[4 * i4 + e][k]), acc[2 * k + 1]);
          }
      }
#pragma unroll
      for (int k = 0; k < 8; ++k) acc[k] = xrow_sum(acc[k]);
      if (r == 0) {
        *(f32x4*)&L.qp[a][8 * c] = f32x4{acc[0], acc[1], acc[2], acc[3]};
        *(f32x4*)&L.qp[a][8 * c + 4] = f32x4{acc[4], acc[5], acc[6], acc[7]};
      }
    }
    wg_barrier();
    {
      float z[8];
#pragma unroll
      for (int k = 0; k < 8; ++k) z[k] = 0.f;
      if (sl < NSL) {
        const int f0 = sl * 32 + a * 8;
        u32x4 g[8], u[8], d2[8];
#pragma unroll
        for (int i = 0; i < 8; ++i) {
          g[i] = *(const u32x4*)(w1 + (size_t)(8 * lane + i) * FF + f0);
          u[i] = *(const u32x4*)(w3 + (size_t)(8 * lane + i) * FF + f0);
          d2[i] = *(const u32x4*)(w2 + (size_t)(f0 + i) * DM + 8 * lane);
        }
        const f32x4 xa = *(const f32x4*)&L.xn[a][8 * lane], xb = *(const f32x4*)&L.xn[a][8 * lane + 4];
        float ga[8], ua[8];
#pragma unroll
        for (int k = 0; k < 8; ++k) { ga[k] = 0.f; ua[k] = 0.f; }
#pragma unroll
        for (int i = 0; i < 8; ++i) {
          const float xi = i < 4 ? xa[i & 3] : xb[i & 3];
#pragma unroll
          for (int k = 0; k < 4; ++k) {
            ga[2 * k] = fmaf(xi, bflo(g[i][k]), ga[2 * k]); ga[2 * k + 1] = fmaf(xi, bfhi(g[i][k]), ga[2 * k + 1]);
            ua[2 * k] = fmaf(xi, bflo(u[i][k]), ua[2 * k]); ua[2 * k + 1] = fmaf(xi, bfhi(u[i][k]), ua[2 * k + 1]);
          }
        }
#pragma unroll
        for (int i = 0; i < 8; ++i) {
          const float gi = rbf(wave_sum(ga[i])), ui = rbf(wave_sum(ua[i]));
          const float hi = rbf(rbf(silu(gi)) * ui);
#pragma unroll
          for (int k = 0; k < 4; ++k) { z[2 * k] = fmaf(hi, bflo(d2[i][k]), z[2 * k]); z[2 * k + 1] = fmaf(hi, bfhi(d2[i][k]), z[2 * k + 1]); }
        }
      }
      *(f32x4*)&L.zp[a][8 * lane] = f32x4{z[0], z[1], z[2], z[3]};
      *(f32x4*)&L.zp[a][8 * lane + 4] = f32x4{z[4], z[5], z[6], z[7]};
    }
    unsigned wp[16][8];
    {
      const unsigned short* p = wo + (size_t)(h * DH + 32 * a) * DM + 8 * lane;
#pragma unroll
      for (int i = 0; i < 32; ++i) wr[i] = *(const u32x4*)(p + (size_t)i * DM);
#pragma unroll
      for (int i = 0; i < 16; ++i)
#pragma unroll
        for (int k = 0; k < 4; ++k) {
          wp[i][2 * k] = __builtin_amdgcn_perm(wr[2 * i + 1][k], wr[2 * i][k], 0x05040100u);
          wp[i][2 * k + 1] = __builtin_amdgcn_perm(wr[2 * i + 1][k], wr[2 * i][k], 0x07060302u);
        }
#pragma unroll
      for (int i = 0; i < 16; ++i)
#pragma unroll
        for (int k = 0; k < 8; ++k) asm volatile("" : "+v"(wp[i][k]));
    }
    wg_barrier();
    {
      float mw[NSW], M = -INFINITY, Ls = 0.f;
#pragma unroll
      for (int s = 0; s < NSW; ++s) { mw[s] = L.op[s][DH]; M = fmaxf(M, mw[s]); }
      float o0 = 0.f, o1 = 0.f;
      const int dd = 32 * a + 2 * (lane & 15);
#pragma unroll
      for (int s = 0; s < NSW; ++s) {
        const float f = __builtin_amdgcn_exp2f((mw[s] - M) * C2);
        Ls = fmaf(f, L.op[s][DH + 1], Ls);
        o0 = fmaf(f, L.op[s][dd], o0); o1 = fmaf(f, L.op[s][dd + 1], o1);
      }
      const float inv = 1.f / Ls;
      const unsigned opk = cvtpk(o0 * inv, o1 * inv);
      float y[8];
#pragma unroll
      for (int k = 0; k < 8; ++k) y[k] = 0.f;
#pragma unroll
      for (int i = 0; i < 16; ++i) {
        const unsigned oi = __builtin_amdgcn_readlane(opk, i);
#pragma unroll
        for (int k = 0; k < 8; ++k) y[k] = dot2(wp[i][k], oi, y[k]);
      }
      *(f32x4*)&L.yp[a][8 * lane] = f32x4{y[0], y[1], y[2], y[3]};
      *(f32x4*)&L.yp[a][8 * lane + 4] = f32x4{y[4], y[5], y[6], y[7]};
      if (a == 0 && lane == 0) *(float2*)(ws + WS_ML + (size_t)slot * 8) = make_float2(M, Ls);
    }
    wg_barrier();
  }
}

__global__ __launch_bounds__(256) void final_reduce(const unsigned short* __restrict__ x, const unsigned char* __restrict__ ws,
                                                    unsigned short* __restrict__ out) {
  __shared__ __attribute__((aligned(16))) float red[4][2][32];
  const int b = blockIdx.x >> 4, cg = blockIdx.x & 15, tid = threadIdx.x, lane = tid & 63, wave = tid >> 6;
  const int hh = tid >> 3, c4 = tid & 7;
  const float* Y = (const float*)(ws + WS_Y) + (size_t)((b * NH + hh) * 2) * DM + cg * 32 + 4 * c4;
  const float* Z = (const float*)(ws + WS_Z) + (size_t)(b * 64 + hh * 2) * DM + cg * 32 + 4 * c4;
  const f32x4 ml = *(const f32x4*)(ws + WS_ML + (size_t)((b * NH + hh) * 2) * 8);
  const f32x4 y0 = *(const f32x4*)Y, y1 = *(const f32x4*)(Y + DM), z0 = *(const f32x4*)Z, z1 = *(const f32x4*)(Z + DM);
  unsigned short xr = 0;
  if (tid < 32) xr = x[b * DM + cg * 32 + tid];
  const float M = fmaxf(ml[0], ml[2]);
  const float a0 = __builtin_amdgcn_exp2f((ml[0] - M) * C2) * ml[1], a1 = __builtin_amdgcn_exp2f((ml[2] - M) * C2) * ml[3];
  const float inv = 1.f / (a0 + a1);
  f32x4 s = y0 * (a0 * inv) + y1 * (a1 * inv), z = z0 + z1;
#pragma unroll
  for (int k = 0; k < 4; ++k) {
    s[k] += dpp<0x128>(s[k]); s[k] = xrow_sum(s[k]);
    z[k] += dpp<0x128>(z[k]); z[k] = xrow_sum(z[k]);
  }
  if (lane < 8) { *(f32x4*)&red[wave][0][4 * lane] = s; *(f32x4*)&red[wave][1][4 * lane] = z; }
  __syncthreads();
  if (tid < 32) {
    const float ss = rbf(red[0][0][tid] + red[1][0][tid] + red[2][0][tid] + red[3][0][tid]);
    const float zz = rbf(red[0][1][tid] + red[1][1][tid] + red[2][1][tid] + red[3][1][tid]);
    const float o = __uint_as_float((unsigned)xr << 16) + rbf(ss * rbf(silu(zz)));
    out[b * DM + cg * 32 + tid] = (unsigned short)(cvtpk(o, o) & 0xffffu);
  }
}

extern "C" void kernel_launch(void* const* d_in, const int* in_sizes, int n_in, void* d_out, int out_size, void* d_ws,
                              size_t ws_size, hipStream_t stream) {
  using cp = const unsigned short*;
  attn_main<<<NWG, 512, 0, stream>>>((cp)d_in[0], (cp)d_in[1], (cp)d_in[2], (cp)d_in[3], (cp)d_in[4], (cp)d_in[6], (cp)d_in[7],
                                     (cp)d_in[8], (unsigned char*)d_ws);
  final_reduce<<<64, 256, 0, stream>>>((cp)d_in[0], (const unsigned char*)d_ws, (unsigned short*)d_out);
}
```

```cpp
#include <hip/hip_runtime.h>
#include <hip/hip_bf16.h>
#include <cstdint>

using u32x4 = __attribute__((ext_vector_type(4))) unsigned;
using f32x4 = __attribute__((ext_vector_type(4))) float;
using bf16x2 = __attribute__((ext_vector_type(2))) __bf16;

constexpr int NB = 4, NH = 32, SK = 8192, DH = 128, DM = 512, FF = 1792;
constexpr int NWG = 256;
constexpr int KEYS = SK / 2;
constexpr int NSL = FF / 32;
#define U_ 4
#define DEPTH_ 1
#define QAUX 2
#define WAUX 2
constexpr int FFNW = 3;
constexpr int U = U_, DEPTH = DEPTH_;
constexpr int NSW = 4;
constexpr int TILE_B = NSW * U * 1024;
constexpr int ITERS = KEYS * DH * 2 / TILE_B;
#define EXTRA_ 2
#define MAP 1
#define AUXFIRST 1
#define QROT 1
constexpr int EXTRA = EXTRA_;
constexpr float C2 = 0.088388347648318440f * 1.4426950408889634f;

constexpr size_t WS_Y = 0, WS_Z = WS_Y + (size_t)NWG * DM * 4, WS_ML = WS_Z + (size_t)NB * 64 * DM * 4;

__device__ __forceinline__ float dot2(unsigned a, unsigned b, float acc) {
  return __builtin_amdgcn_fdot2_f32_bf16(__builtin_bit_cast(bf16x2, a), __builtin_bit_cast(bf16x2, b), acc, false);
}
template <int CTRL> __device__ __forceinline__ float dpp(float x) {
  return __builtin_bit_cast(float, __builtin_amdgcn_mov_dpp(__builtin_bit_cast(int, x), CTRL, 0xf, 0xf, true));
}
__device__ __forceinline__ float row16_sum(float x) {
  x += dpp<0x128>(x); x += dpp<0x124>(x); x += dpp<0x122>(x); x += dpp<0x121>(x); return x;
}
__device__ __forceinline__ float xrow_sum(float x) {
  auto s = __builtin_amdgcn_permlane16_swap(__float_as_uint(x), __float_as_uint(x), false, false);
  x = __uint_as_float(s[0]) + __uint_as_float(s[1]);
  auto t = __builtin_amdgcn_permlane32_swap(__float_as_uint(x), __float_as_uint(x), false, false);
  return __uint_as_float(t[0]) + __uint_as_float(t[1]);
}
__device__ __forceinline__ float xrow_max(float x) {
  auto s = __builtin_amdgcn_permlane16_swap(__float_as_uint(x), __float_as_uint(x), false, false);
  x = fmaxf(__uint_as_float(s[0]), __uint_as_float(s[1]));
  auto t = __builtin_amdgcn_permlane32_swap(__float_as_uint(x), __float_as_uint(x), false, false);
  return fmaxf(__uint_as_float(t[0]), __uint_as_float(t[1]));
}
__device__ __forceinline__ float wave_sum(float x) { return xrow_sum(row16_sum(x)); }
__device__ __forceinline__ float bflo(unsigned w) { return __uint_as_float(w << 16); }
__device__ __forceinline__ float bfhi(unsigned w) { return __uint_as_float(w & 0xffff0000u); }
__device__ __forceinline__ unsigned cvtpk(float lo, float hi) {
  unsigned r; asm("v_cvt_pk_bf16_f32 %0, %1, %2" : "=v"(r) : "v"(lo), "v"(hi)); return r;
}
__device__ __forceinline__ float rbf(float x) { return bflo(cvtpk(x, x)); }
__device__ __forceinline__ __amdgpu_buffer_rsrc_t mkrsrc(const void* p, unsigned bytes) {
  return __builtin_amdgcn_make_buffer_rsrc(const_cast<void*>(p), 0, bytes, 0x00020000);
}
__device__ __forceinline__ void wg_barrier() { asm volatile("s_waitcnt lgkmcnt(0)\n\ts_barrier" ::: "memory"); }
__device__ __forceinline__ float silu(float x) { return x / (1.f + __expf(-x)); }

#define STAMP(k) do {} while (0)
#define SB() __builtin_amdgcn_sched_barrier(0)
struct Lds {
  float xn[4][DM];
  float qp[4][DH];
  float op[NSW][DH + 4];
  float yp[4][DM];
};

__global__ __launch_bounds__(512) void attn_main(const unsigned short* __restrict__ x, const unsigned short* __restrict__ kc,
    const unsigned short* __restrict__ vc, const unsigned short* __restrict__ wq, const unsigned short* __restrict__ wo,
    const unsigned short* __restrict__ w1, const unsigned short* __restrict__ w3, const unsigned short* __restrict__ w2,
    unsigned char* __restrict__ ws) {
  __shared__ __attribute__((aligned(16))) Lds L;
  const int w = blockIdx.x, j = w >> 3;
  const int h = (w & 7) * 4 + (j >> 3), b = (j & 7) >> 1, half = j & 1, qr = QROT ? 4 * (j & 7) : 0;
  const int tid = threadIdx.x ^ (AUXFIRST ? 256 : 0), lane = tid & 63, wave = __builtin_amdgcn_readfirstlane(tid >> 6);
  const int r = lane >> 4, c = lane & 15;
  const int slot = (b * NH + h) * 2 + half;
  const int sl = h * 2 + half;
  if (wave == 0) STAMP(0);
  if (wave < NSW) {
    const size_t kvoff = ((size_t)(b * NH + h) * SK + (size_t)half * KEYS) * DH;
    const auto rk = mkrsrc(kc + kvoff, KEYS * DH * 2), rv = mkrsrc(vc + kvoff, KEYS * DH * 2);
    const unsigned vo = lane * 16 + wave * U * 1024;
    const int rot = (w & 63) * ITERS / 64;
    auto toff = [&](int t) -> unsigned { return t < ITERS ? (unsigned)((t + rot) & (ITERS - 1)) * TILE_B : 0x40000000u; };
    u32x4 kb[DEPTH][U], vb[DEPTH][U], ke[EXTRA + 1][U], ve[EXTRA + 1][U];
#pragma unroll
    for (int d = 0; d < DEPTH; ++d) {
#pragma unroll
      for (int u = 0; u < U; ++u) kb[d][u] = __builtin_amdgcn_raw_buffer_load_b128(rk, vo + u * 1024, toff(d), 2);
#pragma unroll
      for (int u = 0; u < U; ++u) vb[d][u] = __builtin_amdgcn_raw_buffer_load_b128(rv, vo + u * 1024, toff(d), 2);
    }
#pragma unroll
    for (int d = 0; d < EXTRA; ++d) {
#pragma unroll
      for (int u = 0; u < U; ++u) ke[d][u] = __builtin_amdgcn_raw_buffer_load_b128(rk, vo + u * 1024, toff(ITERS - EXTRA + d), 2);
#pragma unroll
      for (int u = 0; u < U; ++u) ve[d][u] = __builtin_amdgcn_raw_buffer_load_b128(rv, vo + u * 1024, toff(ITERS - EXTRA + d), 2);
    }
    wg_barrier();
    if (wave == 0) STAMP(1);
    unsigned q[4];
    {
      f32x4 a0 = {0, 0, 0, 0}, a1 = {0, 0, 0, 0};
#pragma unroll
      for (int a = 0; a < 4; ++a) { a0 += *(const f32x4*)&L.qp[a][8 * c]; a1 += *(const f32x4*)&L.qp[a][8 * c + 4]; }
      q[0] = cvtpk(a0[0], a0[1]); q[1] = cvtpk(a0[2], a0[3]); q[2] = cvtpk(a1[0], a1[1]); q[3] = cvtpk(a1[2], a1[3]);
    }
    float m = -INFINITY, l = 0.f, acc[8];
#pragma unroll
    for (int k = 0; k < 8; ++k) acc[k] = 0.f;
    auto tile = [&](u32x4 (&kt)[U], u32x4 (&vt)[U], const bool refill, const unsigned so) __attribute__((always_inline)) {
      float s[U];
#pragma unroll
      for (int u = 0; u < U; ++u) {
        float t = dot2(kt[u][0], q[0], 0.f);
        t = dot2(kt[u][1], q[1], t); t = dot2(kt[u][2], q[2], t); s[u] = dot2(kt[u][3], q[3], t);
      }
#pragma unroll
      for (int u = 0; u < U; ++u) asm volatile("" : "+v"(s[u]));
      SB();
      if (refill) {
#pragma unroll
        for (int u = 0; u < U; ++u) kt[u] = __builtin_amdgcn_raw_buffer_load_b128(rk, vo + u * 1024, so, 2);
      }
      SB();
      float mx = m;
#pragma unroll
      for (int u = 0; u < U; ++u) { s[u] = row16_sum(s[u]); mx = fmaxf(mx, s[u]); }
      const float mc = -mx * C2;
      const float alpha = __builtin_amdgcn_exp2f(fmaf(m, C2, mc));
      m = mx;
      float ps = 0.f;
#pragma unroll
      for (int u = 0; u < U; ++u) { s[u] = __builtin_amdgcn_exp2f(fmaf(s[u], C2, mc)); ps += s[u]; }
      l = fmaf(l, alpha, ps);
#pragma unroll
      for (int k = 0; k < 8; ++k) acc[k] *= alpha;
#pragma unroll
      for (int u = 0; u < U; ++u) {
#pragma unroll
        for (int k = 0; k < 4; ++k) {
          acc[2 * k] = fmaf(s[u], bflo(vt[u][k]), acc[2 * k]);
          acc[2 * k + 1] = fmaf(s[u], bfhi(vt[u][k]), acc[2 * k + 1]);
        }
      }
#pragma unroll
      for (int k = 0; k < 8; ++k) asm volatile("" : "+v"(acc[k]));
      SB();
      if (refill) {
#pragma unroll
        for (int u = 0; u < U; ++u) vt[u] = __builtin_amdgcn_raw_buffer_load_b128(rv, vo + u * 1024, so, 2);
      }
      SB();
    };
    constexpr int NRING = ITERS - EXTRA;
    static_assert(NRING % DEPTH == 0 && EXTRA * DEPTH <= NRING, "");
#pragma unroll
    for (int e = 0; e < EXTRA; ++e) {
#pragma unroll
      for (int d = 0; d < DEPTH; ++d) tile(kb[d], vb[d], true, toff(e * DEPTH + d + DEPTH));
      tile(ke[e], ve[e], false, 0);
    }
    for (int it = EXTRA * DEPTH; it < NRING; it += DEPTH) {
#pragma unroll
      for (int d = 0; d < DEPTH; ++d) tile(kb[d], vb[d], true, it + d + DEPTH < NRING ? toff(it + d + DEPTH) : 0x40000000u);
    }
    if (wave == 0) STAMP(2);
    const float M = xrow_max(m), f = __builtin_amdgcn_exp2f((m - M) * C2);
    l = xrow_sum(l * f);
#pragma unroll
    for (int k = 0; k < 8; ++k) acc[k] = xrow_sum(acc[k] * f);
    if (r == 0) {
      *(f32x4*)&L.op[wave][8 * c] = f32x4{acc[0], acc[1], acc[2], acc[3]};
      *(f32x4*)&L.op[wave][8 * c + 4] = f32x4{acc[4], acc[5], acc[6], acc[7]};
      if (c == 0) { L.op[wave][DH] = M; L.op[wave][DH + 1] = l; }
    }
    wg_barrier();
    if (wave == 0) STAMP(3);
    wg_barrier();
    if (wave == 0) STAMP(4);
    if (tid < 128) {
      f32x4 y = *(const f32x4*)&L.yp[0][4 * tid];
#pragma unroll
      for (int a = 1; a < 4; ++a) y += *(const f32x4*)&L.yp[a][4 * tid];
      *(f32x4*)(ws + WS_Y + ((size_t)slot * DM + 4 * tid) * 4) = y;
    }
  } else {
    const int a = wave - NSW;
    const u32x4 xv = *(const u32x4*)(x + b * DM + lane * 8);
    u32x4 wr[32];
    {
      const auto rq = mkrsrc(wq, DM * NH * DH * 2);
      const unsigned qo = (unsigned)((128 * a + 32 * r) * (NH * DH) + h * DH + 8 * c) * 2;
#pragma unroll
      for (int i = 0; i < 32; ++i) wr[i] = __builtin_amdgcn_raw_buffer_load_b128(rq, qo, ((i + qr) & 31) * NH * DH * 2, QAUX);
      SB();
    }
    {
      float xf[8];
#pragma unroll
      for (int k = 0; k < 4; ++k) { xf[2 * k] = bflo(xv[k]); xf[2 * k + 1] = bfhi(xv[k]); }
      float s = 0.f;
#pragma unroll
      for (int k = 0; k < 8; ++k) s += xf[k];
      const float mean = wave_sum(s) * (1.f / DM);
      float v = 0.f;
#pragma unroll
      for (int k = 0; k < 8; ++k) { xf[k] -= mean; v = fmaf(xf[k], xf[k], v); }
      const float rs = 1.f / sqrtf(wave_sum(v) * (1.f / DM) + 1e-5f);
      *(f32x4*)&L.xn[a][8 * lane] = f32x4{rbf(xf[0] * rs), rbf(xf[1] * rs), rbf(xf[2] * rs), rbf(xf[3] * rs)};
      *(f32x4*)&L.xn[a][8 * lane + 4] = f32x4{rbf(xf[4] * rs), rbf(xf[5] * rs), rbf(xf[6] * rs), rbf(xf[7] * rs)};
    }
    __builtin_amdgcn_wave_barrier();
    {
      float acc[8];
#pragma unroll
      for (int k = 0; k < 8; ++k) acc[k] = 0.f;
#pragma unroll
      for (int i4 = 0; i4 < 8; ++i4) {
        const f32x4 xn = *(const f32x4*)&L.xn[a][128 * a + 32 * r + ((4 * i4 + qr) & 31)];
#pragma unroll
        for (int e = 0; e < 4; ++e)
#pragma unroll
          for (int k = 0; k < 4; ++k) {
            acc[2 * k] = fmaf(xn[e], bflo(wr[4 * i4 + e][k]), acc[2 * k]);
            acc[2 * k + 1] = fmaf(xn[e], bfhi(wr[4 * i4 + e][k]), acc[2 * k + 1]);
          }
      }
#pragma unroll
      for (int k = 0; k < 8; ++k) acc[k] = xrow_sum(acc[k]);
      if (r == 0) {
        *(f32x4*)&L.qp[a][8 * c] = f32x4{acc[0], acc[1], acc[2], acc[3]};
        *(f32x4*)&L.qp[a][8 * c + 4] = f32x4{acc[4], acc[5], acc[6], acc[7]};
      }
    }
    wg_barrier();
    if (a == FFNW && sl < NSL) {
      const int rr = lane >> 2, cc = lane & 3, f0 = sl * 32;
      float ga[8], ua[8];
#pragma unroll
      for (int k = 0; k < 8; ++k) { ga[k] = 0.f; ua[k] = 0.f; }
      const auto r1 = mkrsrc(w1, DM * FF * 2), r3 = mkrsrc(w3, DM * FF * 2), r2 = mkrsrc(w2, DM * FF * 2);
      const unsigned fo = (unsigned)(32 * rr * FF + f0 + 8 * cc) * 2;
#pragma unroll 1
      for (int i0 = 0; i0 < 32; i0 += 8) {
        u32x4 g[8], u[8];
#pragma unroll
        for (int i = 0; i < 8; ++i) {
          g[i] = __builtin_amdgcn_raw_buffer_load_b128(r1, fo, (i0 + i) * FF * 2, WAUX);
          u[i] = __builtin_amdgcn_raw_buffer_load_b128(r3, fo, (i0 + i) * FF * 2, WAUX);
        }
        const f32x4 xa = *(const f32x4*)&L.xn[a][32 * rr + i0], xb = *(const f32x4*)&L.xn[a][32 * rr + i0 + 4];
#pragma unroll
        for (int i = 0; i < 8; ++i) {
          const float xi = i < 4 ? xa[i & 3] : xb[i & 3];
#pragma unroll
          for (int k = 0; k < 4; ++k) {
            ga[2 * k] = fmaf(xi, bflo(g[i][k]), ga[2 * k]); ga[2 * k + 1] = fmaf(xi, bfhi(g[i][k]), ga[2 * k + 1]);
            ua[2 * k] = fmaf(xi, bflo(u[i][k]), ua[2 * k]); ua[2 * k + 1] = fmaf(xi, bfhi(u[i][k]), ua[2 * k + 1]);
          }
        }
      }
      float hv[8];
#pragma unroll
      for (int k = 0; k < 8; ++k) {
        float gi = ga[k], ui = ua[k];
        gi += dpp<0x128>(gi); gi += dpp<0x124>(gi); gi = rbf(xrow_sum(gi));
        ui += dpp<0x128>(ui); ui += dpp<0x124>(ui); ui = rbf(xrow_sum(ui));
        hv[k] = rbf(rbf(silu(gi)) * ui);
      }
      float z[8];
#pragma unroll
      for (int k = 0; k < 8; ++k) z[k] = 0.f;
#pragma unroll
      for (int c2 = 0; c2 < 4; c2 += 2) {
        u32x4 d2[16];
#pragma unroll
        for (int i = 0; i < 16; ++i) d2[i] = __builtin_amdgcn_raw_buffer_load_b128(r2, lane * 16, (f0 + 8 * c2 + i) * DM * 2, WAUX);
#pragma unroll
        for (int i = 0; i < 16; ++i) {
          const float hi = __uint_as_float(__builtin_amdgcn_readlane(__float_as_uint(hv[i & 7]), c2 + (i >> 3)));
#pragma unroll
          for (int k = 0; k < 4; ++k) { z[2 * k] = fmaf(hi, bflo(d2[i][k]), z[2 * k]); z[2 * k + 1] = fmaf(hi, bfhi(d2[i][k]), z[2 * k + 1]); }
#pragma unroll
          for (int k = 0; k < 8; ++k) asm volatile("" : "+v"(z[k]));
        }
        SB();
      }
      float* zg = (float*)(ws + WS_Z) + (size_t)(b * 64 + sl) * DM + 8 * lane;
      *(f32x4*)zg = f32x4{z[0], z[1], z[2], z[3]};
      *(f32x4*)(zg + 4) = f32x4{z[4], z[5], z[6], z[7]};
      SB();
    }
    unsigned wp[16][8];
    {
      const auto ro = mkrsrc(wo + (size_t)(h * DH + 32 * a) * DM, 32 * DM * 2);
#pragma unroll
      for (int i0 = 0; i0 < 16; i0 += 8) {
        u32x4 t[16];
#pragma unroll
        for (int i = 0; i < 16; ++i) t[i] = __builtin_amdgcn_raw_buffer_load_b128(ro, lane * 16 + (i & 3) * 1024, (2 * i0 + (i & ~3)) * 1024, WAUX);
#pragma unroll
        for (int i = 0; i < 8; ++i)
#pragma unroll
          for (int k = 0; k < 4; ++k) {
            wp[i0 + i][2 * k] = __builtin_amdgcn_perm(t[2 * i + 1][k], t[2 * i][k], 0x05040100u);
            wp[i0 + i][2 * k + 1] = __builtin_amdgcn_perm(t[2 * i + 1][k], t[2 * i][k], 0x07060302u);
          }
#pragma unroll
        for (int i = 0; i < 8; ++i)
#pragma unroll
          for (int k = 0; k < 8; ++k) asm volatile("" : "+v"(wp[i0 + i][k]));
        SB();
      }
    }
    if (a == FFNW) STAMP(5);
    wg_barrier();
    {
      float mw[NSW], M = -INFINITY, Ls = 0.f;
#pragma unroll
      for (int s = 0; s < NSW; ++s) { mw[s] = L.op[s][DH]; M = fmaxf(M, mw[s]); }
      float o0 = 0.f, o1 = 0.f;
      const int dd = 32 * a + 2 * (lane & 15);
#pragma unroll
      for (int s = 0; s < NSW; ++s) {
        const float f = __builtin_amdgcn_exp2f((mw[s] - M) * C2);
        Ls = fmaf(f, L.op[s][DH + 1], Ls);
        o0 = fmaf(f, L.op[s][dd], o0); o1 = fmaf(f, L.op[s][dd + 1], o1);
      }
      const float inv = 1.f / Ls;
      const unsigned opk = cvtpk(o0 * inv, o1 * inv);
      float y[8];
#pragma unroll
      for (int k = 0; k < 8; ++k) y[k] = 0.f;
#pragma unroll
      for (int i = 0; i < 16; ++i) {
        const unsigned oi = __builtin_amdgcn_readlane(opk, i);
#pragma unroll
        for (int k = 0; k < 8; ++k) y[k] = dot2(wp[i][k], oi, y[k]);
      }
      *(f32x4*)&L.yp[a][8 * lane] = f32x4{y[0], y[1], y[2], y[3]};
      *(f32x4*)&L.yp[a][8 * lane + 4] = f32x4{y[4], y[5], y[6], y[7]};
      if (a == 0 && lane == 0) *(float2*)(ws + WS_ML + (size_t)slot * 8) = make_float2(M, Ls);
    }
    wg_barrier();
  }
}

__global__ __launch_bounds__(256) void final_reduce(const unsigned short* __restrict__ x, const unsigned char* __restrict__ ws,
                                                    unsigned short* __restrict__ out) {
  __shared__ __attribute__((aligned(16))) float red[4][2][32];
  const int b = blockIdx.x >> 4, cg = blockIdx.x & 15, tid = threadIdx.x, lane = tid & 63, wave = tid >> 6;
  const int hh = tid >> 3, c4 = tid & 7;
  const float* Y = (const float*)(ws + WS_Y) + (size_t)((b * NH + hh) * 2) * DM + cg * 32 + 4 * c4;
  const float* Z = (const float*)(ws + WS_Z) + (size_t)(b * 64 + hh * 2) * DM + cg * 32 + 4 * c4;
  const f32x4 ml = *(const f32x4*)(ws + WS_ML + (size_t)((b * NH + hh) * 2) * 8);
  const f32x4 y0 = *(const f32x4*)Y, y1 = *(const f32x4*)(Y + DM), z0 = hh < NSL / 2 ? *(const f32x4*)Z : f32x4{0, 0, 0, 0}, z1 = hh < NSL / 2 ? *(const f32x4*)(Z + DM) : f32x4{0, 0, 0, 0};
  unsigned short xr = 0;
  if (tid < 32) xr = x[b * DM + cg * 32 + tid];
  const float M = fmaxf(ml[0], ml[2]);
  const float a0 = __builtin_amdgcn_exp2f((ml[0] - M) * C2) * ml[1], a1 = __builtin_amdgcn_exp2f((ml[2] - M) * C2) * ml[3];
  const float inv = 1.f / (a0 + a1);
  f32x4 s = y0 * (a0 * inv) + y1 * (a1 * inv), z = z0 + z1;
#pragma unroll
  for (int k = 0; k < 4; ++k) {
    s[k] += dpp<0x128>(s[k]); s[k] = xrow_sum(s[k]);
    z[k] += dpp<0x128>(z[k]); z[k] = xrow_sum(z[k]);
  }
  if (lane < 8) { *(f32x4*)&red[wave][0][4 * lane] = s; *(f32x4*)&red[wave][1][4 * lane] = z; }
  __syncthreads();
  if (tid < 32) {
    const float ss = rbf(red[0][0][tid] + red[1][0][tid] + red[2][0][tid] + red[3][0][tid]);
    const float zz = rbf(red[0][1][tid] + red[1][1][tid] + red[2][1][tid] + red[3][1][tid]);
    const float o = __uint_as_float((unsigned)xr << 16) + rbf(ss * rbf(silu(zz)));
    out[b * DM + cg * 32 + tid] = (unsigned short)(cvtpk(o, o) & 0xffffu);
  }
}

extern "C" void kernel_launch(void* const* d_in, const int* in_sizes, int n_in, void* d_out, int out_size, void* d_ws,
                              size_t ws_size, hipStream_t stream) {
  using cp = const unsigned short*;
  attn_main<<<NWG, 512, 0, stream>>>((cp)d_in[0], (cp)d_in[1], (cp)d_in[2], (cp)d_in[3], (cp)d_in[4], (cp)d_in[6], (cp)d_in[7],
                                     (cp)d_in[8], (unsigned char*)d_ws);
  final_reduce<<<64, 256, 0, stream>>>((cp)d_in[0], (const unsigned char*)d_ws, (unsigned short*)d_out);
}
```

```cpp
#include <hip/hip_runtime.h>
#include <hip/hip_bf16.h>
#include <cstdint>

using u32x4 = __attribute__((ext_vector_type(4))) unsigned;
using f32x4 = __attribute__((ext_vector_type(4))) float;
using bf16x2 = __attribute__((ext_vector_type(2))) __bf16;

constexpr int NB = 4, NH = 32, SK = 8192, DH = 128, DM = 512, FF = 1792;
constexpr int NWG = 256;
constexpr int KEYS = SK / 2;
constexpr int NSL = FF / 32;
constexpr int KAUX = 2, VAUX = 2;
constexpr int QAUX = 0, WAUX = 0;
constexpr int SAUX = 16, GAUX = 16;
constexpr int PTHR = 8, PSLEEP = 8;
constexpr int FFNW = 3;
constexpr int U = 4, DEPTH = 1;
constexpr int NSW = 4;
constexpr int TILE_B = NSW * U * 1024;
constexpr int ITERS = KEYS * DH * 2 / TILE_B;
constexpr int EXTRA = 2;
constexpr float C2 = 0.088388347648318440f * 1.4426950408889634f;

constexpr unsigned WS_Y = 0, WS_Z = WS_Y + NWG * DM * 8, WS_ML = WS_Z + NB * 64 * DM * 8, WS_E = WS_ML + NWG * 16, WS_END = WS_E + NB * 128;
constexpr int SPIN_CAP = 1 << 20;
using u32x2 = __attribute__((ext_vector_type(2))) unsigned;
using gu64 = __attribute__((address_space(1))) unsigned long long;
#define RLX_AGENT __ATOMIC_RELAXED, __HIP_MEMORY_SCOPE_AGENT

__device__ __forceinline__ float dot2(unsigned a, unsigned b, float acc) {
  return __builtin_amdgcn_fdot2_f32_bf16(__builtin_bit_cast(bf16x2, a), __builtin_bit_cast(bf16x2, b), acc, false);
}
template <int CTRL> __device__ __forceinline__ float dpp(float x) {
  return __builtin_bit_cast(float, __builtin_amdgcn_mov_dpp(__builtin_bit_cast(int, x), CTRL, 0xf, 0xf, true));
}
__device__ __forceinline__ float row16_sum(float x) {
  x += dpp<0x128>(x); x += dpp<0x124>(x); x += dpp<0x122>(x); x += dpp<0x121>(x); return x;
}
__device__ __forceinline__ float xrow_sum(float x) {
  auto s = __builtin_amdgcn_permlane16_swap(__float_as_uint(x), __float_as_uint(x), false, false);
  x = __uint_as_float(s[0]) + __uint_as_float(s[1]);
  auto t = __builtin_amdgcn_permlane32_swap(__float_as_uint(x), __float_as_uint(x), false, false);
  return __uint_as_float(t[0]) + __uint_as_float(t[1]);
}
__device__ __forceinline__ float xrow_max(float x) {
  auto s = __builtin_amdgcn_permlane16_swap(__float_as_uint(x), __float_as_uint(x), false, false);
  x = fmaxf(__uint_as_float(s[0]), __uint_as_float(s[1]));
  auto t = __builtin_amdgcn_permlane32_swap(__float_as_uint(x), __float_as_uint(x), false, false);
  return fmaxf(__uint_as_float(t[0]), __uint_as_float(t[1]));
}
__device__ __forceinline__ float wave_sum(float x) { return xrow_sum(row16_sum(x)); }
__device__ __forceinline__ float bflo(unsigned w) { return __uint_as_float(w << 16); }
__device__ __forceinline__ float bfhi(unsigned w) { return __uint_as_float(w & 0xffff0000u); }
__device__ __forceinline__ unsigned cvtpk(float lo, float hi) {
  unsigned r; asm("v_cvt_pk_bf16_f32 %0, %1, %2" : "=v"(r) : "v"(lo), "v"(hi)); return r;
}
__device__ __forceinline__ float rbf(float x) { return bflo(cvtpk(x, x)); }
__device__ __forceinline__ __amdgpu_buffer_rsrc_t mkrsrc(const void* p, unsigned bytes) {
  return __builtin_amdgcn_make_buffer_rsrc(const_cast<void*>(p), 0, bytes, 0x00020000);
}
__device__ __forceinline__ void wg_barrier() { asm volatile("s_waitcnt lgkmcnt(0)\n\ts_barrier" ::: "memory"); }
__device__ __forceinline__ float silu(float x) { return x / (1.f + __expf(-x)); }

#define SB() __builtin_amdgcn_sched_barrier(0)
struct Lds {
  float xn[4][DM];
  float qp[4][DH];
  float op[NSW][DH + 4];
  float yp[4][DM];
  unsigned tag[2];
};

__global__ __launch_bounds__(512, 2) void attn_main(const unsigned short* __restrict__ x, const unsigned short* __restrict__ kc,
    const unsigned short* __restrict__ vc, const unsigned short* __restrict__ wq, const unsigned short* __restrict__ wo,
    const unsigned short* __restrict__ w1, const unsigned short* __restrict__ w3, const unsigned short* __restrict__ w2,
    unsigned char* ws, unsigned short* __restrict__ out) {
  __shared__ __attribute__((aligned(16))) Lds L;
  const int w = blockIdx.x, j = w >> 3;
  const int h = (w & 7) * 4 + (j >> 3), b = (j & 7) >> 1, half = j & 1, qr = 4 * (j & 7);
  const int tid = threadIdx.x ^ 256, lane = tid & 63, wave = __builtin_amdgcn_readfirstlane(tid >> 6);
  const int r = lane >> 4, c = lane & 15;
  const int slot = (b * NH + h) * 2 + half;
  const int sl = h * 2 + half;
  if (wave < NSW) {
    const size_t kvoff = ((size_t)(b * NH + h) * SK + (size_t)half * KEYS) * DH;
    const auto rk = mkrsrc(kc + kvoff, KEYS * DH * 2), rv = mkrsrc(vc + kvoff, KEYS * DH * 2);
    const unsigned vo = lane * 16 + wave * U * 1024;
    const int rot = (w & 63) * ITERS / 64;
    auto toff = [&](int t) -> unsigned { return t < ITERS ? (unsigned)((t + rot) & (ITERS - 1)) * TILE_B : 0x40000000u; };
    u32x4 kb[DEPTH][U], vb[DEPTH][U], ke[EXTRA + 1][U], ve[EXTRA + 1][U];
#pragma unroll
    for (int d = 0; d < DEPTH; ++d) {
#pragma unroll
      for (int u = 0; u < U; ++u) kb[d][u] = __builtin_amdgcn_raw_buffer_load_b128(rk, vo + u * 1024, toff(d), KAUX);
#pragma unroll
      for (int u = 0; u < U; ++u) vb[d][u] = __builtin_amdgcn_raw_buffer_load_b128(rv, vo + u * 1024, toff(d), VAUX);
    }
#pragma unroll
    for (int d = 0; d < EXTRA; ++d) {
#pragma unroll
      for (int u = 0; u < U; ++u) ke[d][u] = __builtin_amdgcn_raw_buffer_load_b128(rk, vo + u * 1024, toff(ITERS - EXTRA + d), KAUX);
#pragma unroll
      for (int u = 0; u < U; ++u) ve[d][u] = __builtin_amdgcn_raw_buffer_load_b128(rv, vo + u * 1024, toff(ITERS - EXTRA + d), VAUX);
    }
    wg_barrier();
    unsigned q[4];
    {
      f32x4 a0 = {0, 0, 0, 0}, a1 = {0, 0, 0, 0};
#pragma unroll
      for (int a = 0; a < 4; ++a) { a0 += *(const f32x4*)&L.qp[a][8 * c]; a1 += *(const f32x4*)&L.qp[a][8 * c + 4]; }
      q[0] = cvtpk(a0[0], a0[1]); q[1] = cvtpk(a0[2], a0[3]); q[2] = cvtpk(a1[0], a1[1]); q[3] = cvtpk(a1[2], a1[3]);
    }
    float m = -INFINITY, l = 0.f, acc[8];
#pragma unroll
    for (int k = 0; k < 8; ++k) acc[k] = 0.f;
    auto tile = [&](u32x4 (&kt)[U], u32x4 (&vt)[U], const bool refill, const unsigned so) __attribute__((always_inline)) {
      float s[U];
#pragma unroll
      for (int u = 0; u < U; ++u) {
        float t = dot2(kt[u][0], q[0], 0.f);
        t = dot2(kt[u][1], q[1], t); t = dot2(kt[u][2], q[2], t); s[u] = dot2(kt[u][3], q[3], t);
      }
#pragma unroll
      for (int u = 0; u < U; ++u) asm volatile("" : "+v"(s[u]));
      SB();
      if (refill) {
#pragma unroll
        for (int u = 0; u < U; ++u) kt[u] = __builtin_amdgcn_raw_buffer_load_b128(rk, vo + u * 1024, so, KAUX);
      }
      SB();
      float mx = m;
#pragma unroll
      for (int u = 0; u < U; ++u) { s[u] = row16_sum(s[u]); mx = fmaxf(mx, s[u]); }
      const float mc = -mx * C2;
      const float alpha = __builtin_amdgcn_exp2f(fmaf(m, C2, mc));
      m = mx;
      float ps = 0.f;
#pragma unroll
      for (int u = 0; u < U; ++u) { s[u] = __builtin_amdgcn_exp2f(fmaf(s[u], C2, mc)); ps += s[u]; }
      l = fmaf(l, alpha, ps);
#pragma unroll
      for (int k = 0; k < 8; ++k) acc[k] *= alpha;
#pragma unroll
      for (int u = 0; u < U; ++u) {
#pragma unroll
        for (int k = 0; k < 4; ++k) {
          acc[2 * k] = fmaf(s[u], bflo(vt[u][k]), acc[2 * k]);
          acc[2 * k + 1] = fmaf(s[u], bfhi(vt[u][k]), acc[2 * k + 1]);
        }
      }
#pragma unroll
      for (int k = 0; k < 8; ++k) asm volatile("" : "+v"(acc[k]));
      SB();
      if (refill) {
#pragma unroll
        for (int u = 0; u < U; ++u) vt[u] = __builtin_amdgcn_raw_buffer_load_b128(rv, vo + u * 1024, so, VAUX);
      }
      SB();
    };
    constexpr int NRING = ITERS - EXTRA;
    static_assert(NRING % DEPTH == 0 && EXTRA * DEPTH <= NRING, "");
#pragma unroll
    for (int e = 0; e < EXTRA; ++e) {
#pragma unroll
      for (int d = 0; d < DEPTH; ++d) tile(kb[d], vb[d], true, toff(e * DEPTH + d + DEPTH));
      tile(ke[e], ve[e], false, 0);
    }
    for (int it = EXTRA * DEPTH; it < NRING; it += DEPTH) {
#pragma unroll
      for (int d = 0; d < DEPTH; ++d) tile(kb[d], vb[d], true, it + d + DEPTH < NRING ? toff(it + d + DEPTH) : 0x40000000u);
    }
    const float M = xrow_max(m), f = __builtin_amdgcn_exp2f((m - M) * C2);
    l = xrow_sum(l * f);
#pragma unroll
    for (int k = 0; k < 8; ++k) acc[k] = xrow_sum(acc[k] * f);
    if (r == 0) {
      *(f32x4*)&L.op[wave][8 * c] = f32x4{acc[0], acc[1], acc[2], acc[3]};
      *(f32x4*)&L.op[wave][8 * c + 4] = f32x4{acc[4], acc[5], acc[6], acc[7]};
      if (c == 0) { L.op[wave][DH] = M; L.op[wave][DH + 1] = l; }
    }
    wg_barrier();
    const u32x2 tg = *(const u32x2*)L.tag;
    if (sl < NSL) {
      const u32x2 zz = *(const u32x2*)&L.xn[0][2 * tid];
      __builtin_amdgcn_raw_buffer_store_b128(u32x4{zz[0], tg[0], zz[1], tg[1]}, mkrsrc(ws, WS_END), WS_Z + slot * DM * 8 + tid * 16, 0, SAUX);
    }
    wg_barrier();
    {
      float y0 = 0.f, y1 = 0.f;
#pragma unroll
      for (int a = 0; a < 4; ++a) { const float2 t = *(const float2*)&L.yp[a][2 * tid]; y0 += t.x; y1 += t.y; }
      __builtin_amdgcn_raw_buffer_store_b128(u32x4{__float_as_uint(y0), tg[0], __float_as_uint(y1), tg[1]}, mkrsrc(ws, WS_END),
                                             WS_Y + slot * DM * 8 + tid * 16, 0, SAUX);
    }
    wg_barrier();
  } else {
    const int a = wave - NSW;
    const u32x4 xv = *(const u32x4*)(x + b * DM + lane * 8);
    u32x4 wr[32];
    {
      const auto rq = mkrsrc(wq, DM * NH * DH * 2);
      const unsigned qo = (unsigned)((128 * a + r) * (NH * DH) + h * DH + 8 * c) * 2;
#pragma unroll
      for (int i = 0; i < 32; ++i) wr[i] = __builtin_amdgcn_raw_buffer_load_b128(rq, qo, ((i + qr) & 31) * 4 * NH * DH * 2, QAUX);
      SB();
    }
    {
      float xf[8];
#pragma unroll
      for (int k = 0; k < 4; ++k) { xf[2 * k] = bflo(xv[k]); xf[2 * k + 1] = bfhi(xv[k]); }
      float s = 0.f;
#pragma unroll
      for (int k = 0; k < 8; ++k) s += xf[k];
      const float mean = wave_sum(s) * (1.f / DM);
      float v = 0.f;
#pragma unroll
      for (int k = 0; k < 8; ++k) { xf[k] -= mean; v = fmaf(xf[k], xf[k], v); }
      const float rs = 1.f / sqrtf(wave_sum(v) * (1.f / DM) + 1e-5f);
#pragma unroll
      for (int k = 0; k < 4; ++k)
        *(float2*)&L.xn[a][128 * r + 32 * k + 2 * c] = make_float2(rbf(xf[k] * rs), rbf(xf[k + 4] * rs));
    }
    __builtin_amdgcn_wave_barrier();
    {
      float acc[8];
#pragma unroll
      for (int k = 0; k < 8; ++k) acc[k] = 0.f;
#pragma unroll
      for (int i4 = 0; i4 < 8; ++i4) {
        const f32x4 xn = *(const f32x4*)&L.xn[a][128 * a + 32 * r + ((4 * i4 + qr) & 31)];
#pragma unroll
        for (int e = 0; e < 4; ++e)
#pragma unroll
          for (int k = 0; k < 4; ++k) {
            acc[2 * k] = fmaf(xn[e], bflo(wr[4 * i4 + e][k]), acc[2 * k]);
            acc[2 * k + 1] = fmaf(xn[e], bfhi(wr[4 * i4 + e][k]), acc[2 * k + 1]);
          }
      }
#pragma unroll
      for (int k = 0; k < 8; ++k) acc[k] = xrow_sum(acc[k]);
      if (r == 0) {
        *(f32x4*)&L.qp[a][8 * c] = f32x4{acc[0], acc[1], acc[2], acc[3]};
        *(f32x4*)&L.qp[a][8 * c + 4] = f32x4{acc[4], acc[5], acc[6], acc[7]};
      }
    }
    wg_barrier();
    const auto rw = mkrsrc(ws, WS_END);
    if (a == 0) {
      const unsigned long long tag = __hip_atomic_load((gu64*)(ws + WS_E + b * 128), RLX_AGENT) * 6364136223846793005ull + 1442695040888963407ull;
      if (lane == 0) *(u32x2*)L.tag = u32x2{(unsigned)tag, (unsigned)(tag >> 32)};
    }
    if (a == FFNW && sl < NSL) {
      const int rr = lane >> 2, cc = lane & 3, f0 = sl * 32;
      float ga[8], ua[8];
#pragma unroll
      for (int k = 0; k < 8; ++k) { ga[k] = 0.f; ua[k] = 0.f; }
      const auto r1 = mkrsrc(w1, DM * FF * 2), r3 = mkrsrc(w3, DM * FF * 2), r2 = mkrsrc(w2, DM * FF * 2);
      const unsigned fo = (unsigned)(rr * FF + f0 + 8 * cc) * 2;
#pragma unroll 1
      for (int i0 = 0; i0 < 32; i0 += 8) {
        u32x4 g[8], u[8];
#pragma unroll
        for (int i = 0; i < 8; ++i) {
          g[i] = __builtin_amdgcn_raw_buffer_load_b128(r1, fo, (i0 + i) * 16 * FF * 2, WAUX);
          u[i] = __builtin_amdgcn_raw_buffer_load_b128(r3, fo, (i0 + i) * 16 * FF * 2, WAUX);
        }
#pragma unroll
        for (int i = 0; i < 8; ++i) {
          const float xi = L.xn[a][16 * i0 + 32 * (rr & 3) + 4 * i + (rr >> 2)];
#pragma unroll
          for (int k = 0; k < 4; ++k) {
            ga[2 * k] = fmaf(xi, bflo(g[i][k]), ga[2 * k]); ga[2 * k + 1] = fmaf(xi, bfhi(g[i][k]), ga[2 * k + 1]);
            ua[2 * k] = fmaf(xi, bflo(u[i][k]), ua[2 * k]); ua[2 * k + 1] = fmaf(xi, bfhi(u[i][k]), ua[2 * k + 1]);
          }
        }
      }
      float hv[8];
#pragma unroll
      for (int k = 0; k < 8; ++k) {
        float gi = ga[k], ui = ua[k];
        gi += dpp<0x128>(gi); gi += dpp<0x124>(gi); gi = rbf(xrow_sum(gi));
        ui += dpp<0x128>(ui); ui += dpp<0x124>(ui); ui = rbf(xrow_sum(ui));
        hv[k] = rbf(rbf(silu(gi)) * ui);
      }
      float z[8];
#pragma unroll
      for (int k = 0; k < 8; ++k) z[k] = 0.f;
#pragma unroll
      for (int c2 = 0; c2 < 4; c2 += 2) {
        u32x4 d2[16];
#pragma unroll
        for (int i = 0; i < 16; ++i) d2[i] = __builtin_amdgcn_raw_buffer_load_b128(r2, lane * 16, (f0 + 8 * c2 + i) * DM * 2, WAUX);
#pragma unroll
        for (int i = 0; i < 16; ++i) {
          const float hi = __uint_as_float(__builtin_amdgcn_readlane(__float_as_uint(hv[i & 7]), c2 + (i >> 3)));
#pragma unroll
          for (int k = 0; k < 4; ++k) { z[2 * k] = fmaf(hi, bflo(d2[i][k]), z[2 * k]); z[2 * k + 1] = fmaf(hi, bfhi(d2[i][k]), z[2 * k + 1]); }
#pragma unroll
          for (int k = 0; k < 8; ++k) asm volatile("" : "+v"(z[k]));
        }
        SB();
      }
      *(f32x4*)&L.xn[0][8 * lane] = f32x4{z[0], z[1], z[2], z[3]};
      *(f32x4*)&L.xn[0][8 * lane + 4] = f32x4{z[4], z[5], z[6], z[7]};
      SB();
    }
    unsigned wp[16][8];
    {
      const auto ro = mkrsrc(wo + (size_t)(h * DH + 32 * a) * DM, 32 * DM * 2);
#pragma unroll
      for (int i0 = 0; i0 < 16; i0 += 8) {
        u32x4 t[16];
#pragma unroll
        for (int i = 0; i < 16; ++i) t[i] = __builtin_amdgcn_raw_buffer_load_b128(ro, lane * 16 + (i & 3) * 1024, (2 * i0 + (i & ~3)) * 1024, WAUX);
#pragma unroll
        for (int i = 0; i < 8; ++i)
#pragma unroll
          for (int k = 0; k < 4; ++k) {
            wp[i0 + i][2 * k] = __builtin_amdgcn_perm(t[2 * i + 1][k], t[2 * i][k], 0x05040100u);
            wp[i0 + i][2 * k + 1] = __builtin_amdgcn_perm(t[2 * i + 1][k], t[2 * i][k], 0x07060302u);
          }
#pragma unroll
        for (int i = 0; i < 8; ++i)
#pragma unroll
          for (int k = 0; k < 8; ++k) asm volatile("" : "+v"(wp[i0 + i][k]));
        SB();
      }
    }
    const int ms = 16 * a + (lane >> 2);
    unsigned xr = *(const unsigned*)(x + b * DM + sl * 8 + (lane & 3) * 2);
    asm volatile("" : "+v"(xr));
    wg_barrier();
    const unsigned t0 = __builtin_amdgcn_readfirstlane(L.tag[0]), t1 = __builtin_amdgcn_readfirstlane(L.tag[1]);
    u32x4 ml;
    {
      float mw[NSW], M = -INFINITY, Ls = 0.f;
#pragma unroll
      for (int s = 0; s < NSW; ++s) { mw[s] = L.op[s][DH]; M = fmaxf(M, mw[s]); }
      float o0 = 0.f, o1 = 0.f;
      const int dd = 32 * a + 2 * (lane & 15);
#pragma unroll
      for (int s = 0; s < NSW; ++s) {
        const float f = __builtin_amdgcn_exp2f((mw[s] - M) * C2);
        Ls = fmaf(f, L.op[s][DH + 1], Ls);
        o0 = fmaf(f, L.op[s][dd], o0); o1 = fmaf(f, L.op[s][dd + 1], o1);
      }
      const u32x4 mlown = {__float_as_uint(M), t0, __float_as_uint(Ls), t1};
      if (a == 0 && lane == 0) __builtin_amdgcn_raw_buffer_store_b128(mlown, rw, WS_ML + slot * 16, 0, SAUX);
      ml = __builtin_amdgcn_raw_buffer_load_b128(rw, WS_ML + (b * 64 + ms) * 16, 0, GAUX);
      SB();
      const float inv = 1.f / Ls;
      const unsigned opk = cvtpk(o0 * inv, o1 * inv);
      float y[8];
#pragma unroll
      for (int k = 0; k < 8; ++k) y[k] = 0.f;
#pragma unroll
      for (int i = 0; i < 16; ++i) {
        const unsigned oi = __builtin_amdgcn_readlane(opk, i);
#pragma unroll
        for (int k = 0; k < 8; ++k) y[k] = dot2(wp[i][k], oi, y[k]);
      }
      *(f32x4*)&L.yp[a][8 * lane] = f32x4{y[0], y[1], y[2], y[3]};
      *(f32x4*)&L.yp[a][8 * lane + 4] = f32x4{y[4], y[5], y[6], y[7]};
      SB();
      wg_barrier();
      if (ms == sl) ml = mlown;
    }
    {
      const unsigned yo = (b * 64 + ms) * DM * 8 + sl * 64 + (lane & 3) * 16;
      bool hm = ml[1] == t0 && ml[3] == t1, hy = false, hz = ms >= NSL;
      u32x4 y = {0, 0, 0, 0}, z = y;
#pragma unroll 1
      for (int spin = 0; spin < SPIN_CAP; ++spin) {
        asm volatile("" ::: "memory");
        if (!hm) ml = __builtin_amdgcn_raw_buffer_load_b128(rw, WS_ML + (b * 64 + ms) * 16, 0, GAUX);
        if (hm && !hz) z = __builtin_amdgcn_raw_buffer_load_b128(rw, yo, WS_Z, GAUX);
        if (hm && !hy) y = __builtin_amdgcn_raw_buffer_load_b128(rw, yo, WS_Y, GAUX);
        hz = hz || (hm && z[1] == t0 && z[3] == t1);
        hy = hy || (hm && y[1] == t0 && y[3] == t1);
        hm = ml[1] == t0 && ml[3] == t1;
        if (__all(hy && hz)) break;
        if (__popcll(__ballot(!hm)) > PTHR) __builtin_amdgcn_s_sleep(PSLEEP);
      }
      const float m = __uint_as_float(ml[0]), l = __uint_as_float(ml[2]);
      const float M = fmaxf(m, dpp<0x141>(m));
      const float e = __builtin_amdgcn_exp2f((m - M) * C2) * l;
      const float wt = e / (e + dpp<0x141>(e));
      f32x4 p = {__uint_as_float(y[0]) * wt, __uint_as_float(y[2]) * wt, __uint_as_float(z[0]), __uint_as_float(z[2])};
#pragma unroll
      for (int k = 0; k < 4; ++k) { float t = p[k]; t += dpp<0x128>(t); t += dpp<0x124>(t); p[k] = xrow_sum(t); }
      if (lane < 4) *(f32x4*)&L.qp[a][4 * lane] = p;
      wg_barrier();
      if (a == 0 && lane < 4) {
#pragma unroll
        for (int k = 1; k < 4; ++k) p += *(const f32x4*)&L.qp[k][4 * lane];
        const float o0 = bflo(xr) + rbf(rbf(p[0]) * rbf(silu(rbf(p[2])))), o1 = bfhi(xr) + rbf(rbf(p[1]) * rbf(silu(rbf(p[3]))));
        *(unsigned*)(out + b * DM + sl * 8 + lane * 2) = cvtpk(o0, o1);
        if (sl == 0 && lane == 0) __hip_atomic_store((gu64*)(ws + WS_E + b * 128), ((unsigned long long)t1 << 32) | t0, RLX_AGENT);
      }
    }
  }
}

extern "C" void kernel_launch(void* const* d_in, const int* in_sizes, int n_in, void* d_out, int out_size, void* d_ws,
                              size_t ws_size, hipStream_t stream) {
  using cp = const unsigned short*;
  attn_main<<<NWG, 512, 0, stream>>>((cp)d_in[0], (cp)d_in[1], (cp)d_in[2], (cp)d_in[3], (cp)d_in[4], (cp)d_in[6], (cp)d_in[7],
                                     (cp)d_in[8], (unsigned char*)d_ws, (unsigned short*)d_out);
}
```
